```python
import torch

def _softmax(x, axis=-1):
    return torch.softmax(x, dim=axis)

def _gelu(x):
    return torch.nn.functional.gelu(x, approximate='tanh')

def _silu(x):
    return torch.nn.functional.silu(x)

def _sigmoid(x):
    return torch.sigmoid(x)

def _layernorm(x, axis=-1, eps=1e-05):
    m = x.mean(dim=axis, keepdim=True)
    v = x.var(dim=axis, keepdim=True, unbiased=False)
    return (x - m) / torch.sqrt(v + eps)

def _scatter_add(tgt, idx, src, axis=0):
    return tgt.clone().index_add_(axis, idx, src)

def _segment_sum(src, seg, n):
    out = torch.zeros((n,) + src.shape[1:], dtype=src.dtype)
    return out.index_add_(0, seg, src)

def _pad(x, pw):
    flat = tuple((v for pair in reversed(pw) for v in pair))
    return torch.nn.functional.pad(x, flat)
kernel_name = 'standalone__tree_speculative_verify_tree_attention_over_candidate_token_tree__decode'

def setup_inputs():

    _w = lambda a: (lambda t: t.to(torch.bfloat16) if t.is_floating_point() else t)(torch.as_tensor(a))
    B, Hq, Hkv, T, C, D = (1, 32, 32, 1, 4096, 128)
    tree_mask = torch.zeros((B, T, T), dtype=float)
    for b in range(B):
        parent = torch.zeros(T, dtype=torch.int64)
        for i in range(1, T):
            parent[i] = int(torch.randint(0, i, (1,)).item())
        for i in range(T):
            j = i
            tree_mask[b, i, j] = 1.0
            while j > 0:
                j = parent[j]
                tree_mask[b, i, j] = 1.0
    return {'q': _w((lambda *s: torch.randn(*s, dtype=torch.bfloat16) * 0.8)(B, Hq, T, D)), 'k_cache': _w((lambda *s: torch.randn(*s, dtype=torch.bfloat16) * 0.8)(B, Hkv, C, D)), 'v_cache': _w((lambda *s: torch.randn(*s, dtype=torch.bfloat16) * 0.05)(B, Hkv, C, D)), 'k_tree': _w((lambda *s: torch.randn(*s, dtype=torch.bfloat16) * 0.8)(B, Hkv, T, D)), 'v_tree': _w((lambda *s: torch.randn(*s, dtype=torch.bfloat16) * 0.05)(B, Hkv, T, D)), 'tree_mask': _w(tree_mask)}

def reference(q, k_cache, v_cache, k_tree, v_tree, tree_mask):
    B, Hq, T, D = q.shape
    Hkv, C = (k_cache.shape[1], k_cache.shape[2])
    rep = Hq // Hkv
    kc = (lambda x, n, axis: torch.repeat_interleave(x, n, dim=axis))(k_cache, rep, axis=1)
    vc = (lambda x, n, axis: torch.repeat_interleave(x, n, dim=axis))(v_cache, rep, axis=1)
    kt = (lambda x, n, axis: torch.repeat_interleave(x, n, dim=axis))(k_tree, rep, axis=1)
    vt = (lambda x, n, axis: torch.repeat_interleave(x, n, dim=axis))(v_tree, rep, axis=1)
    k = torch.concatenate([kc, kt], axis=2)
    v = torch.concatenate([vc, vt], axis=2)
    scale = D ** (-0.5)
    scores = torch.einsum('bhqd,bhkd->bhqk', q, k) * scale
    ctx_mask = torch.ones((B, 1, T, C), dtype=scores.dtype)
    tm = tree_mask[:, None, :, :].clone()
    full_mask = torch.concatenate([ctx_mask, tm], axis=-1)
    scores = scores + (1.0 - full_mask) * -1000000000.0
    p = _softmax(scores, axis=-1)
    return torch.einsum('bhqk,bhkd->bhqd', p, v)
```

```cpp
#include <hip/hip_runtime.h>
#include <hip/hip_bf16.h>
#include <stdint.h>

constexpr int H = 32, C = 4096, D = 128;
constexpr int NSPLIT = 8, WAVES = 8, NTHR = WAVES * 64;
constexpr int NLD = 16;
constexpr int NPRE = 6;
static_assert(NSPLIT * WAVES * NLD * 4 == C && D == 128, "layout");
constexpr float SCALE = 0.088388347648318440f;
constexpr float LOG2E = 1.4426950408889634f;
constexpr int NT = 2, SC1 = 16;
constexpr int SPIN = 1 << 22;
constexpr unsigned WS_EPOCH = 0, WS_ML = 16384, WS_ACC = 32768, WS_END = WS_ACC + H * NSPLIT * 1024;

using bf16x2 = __attribute__((ext_vector_type(2))) __bf16;
using bf16x8 = __attribute__((ext_vector_type(8))) __bf16;
using u32x4 = __attribute__((ext_vector_type(4))) unsigned;
using f32x4 = __attribute__((ext_vector_type(4))) float;
using f32x2 = __attribute__((ext_vector_type(2))) float;
typedef __attribute__((address_space(1))) unsigned gu32;
#define RLX_AGENT __ATOMIC_RELAXED, __HIP_MEMORY_SCOPE_AGENT
#define SB() __builtin_amdgcn_sched_barrier(0)

__device__ __forceinline__ float dot2(unsigned k, unsigned q, float acc) {
  return __builtin_amdgcn_fdot2_f32_bf16(__builtin_bit_cast(bf16x2, k), __builtin_bit_cast(bf16x2, q), acc, false);
}
template <int CTRL> __device__ __forceinline__ unsigned dppu(unsigned x) {
  return (unsigned)__builtin_amdgcn_mov_dpp((int)x, CTRL, 0xf, 0xf, true);
}
template <int CTRL> __device__ __forceinline__ float dpp(float x) { return __uint_as_float(dppu<CTRL>(__float_as_uint(x))); }
constexpr int XOR1 = 0xB1, XOR2 = 0x4E, XOR7 = 0x141, XOR8 = 0x128, BCAST = 0x150;
__device__ __forceinline__ float wave_max(float x) {
  x = fmaxf(x, dpp<XOR8>(x)); x = fmaxf(x, dpp<XOR7>(x)); x = fmaxf(x, dpp<XOR2>(x)); x = fmaxf(x, dpp<XOR1>(x));
  auto s = __builtin_amdgcn_permlane16_swap(__float_as_uint(x), __float_as_uint(x), false, false);
  x = fmaxf(__uint_as_float(s[0]), __uint_as_float(s[1]));
  auto t = __builtin_amdgcn_permlane32_swap(__float_as_uint(x), __float_as_uint(x), false, false);
  return fmaxf(__uint_as_float(t[0]), __uint_as_float(t[1]));
}
__device__ __forceinline__ float wave_sum(float x) {
  x += dpp<XOR8>(x); x += dpp<XOR7>(x); x += dpp<XOR2>(x); x += dpp<XOR1>(x);
  auto s = __builtin_amdgcn_permlane16_swap(__float_as_uint(x), __float_as_uint(x), false, false);
  x = __uint_as_float(s[0]) + __uint_as_float(s[1]);
  auto t = __builtin_amdgcn_permlane32_swap(__float_as_uint(x), __float_as_uint(x), false, false);
  return __uint_as_float(t[0]) + __uint_as_float(t[1]);
}
__device__ __forceinline__ float bflo(unsigned v) { return __uint_as_float(v << 16); }
__device__ __forceinline__ float bfhi(unsigned v) { return __uint_as_float(v & 0xffff0000u); }
__device__ __forceinline__ unsigned cvtpk(float lo, float hi) {
  const bf16x2 r = {(__bf16)lo, (__bf16)hi};
  return __builtin_bit_cast(unsigned, r);
}
__device__ __forceinline__ float ex2(float x) { return __builtin_amdgcn_exp2f(x); }
__device__ __forceinline__ void wg_barrier() {
  asm volatile("s_waitcnt lgkmcnt(0)" ::: "memory");
  __builtin_amdgcn_s_barrier();
  asm volatile("" ::: "memory");
}
__device__ __forceinline__ f32x2 merge8(const float* lds, int lane) {
  f32x2 a[WAVES];
#pragma unroll
  for (int w = 0; w < WAVES; ++w) a[w] = *reinterpret_cast<const f32x2*>(&lds[w * D + 2 * lane]);
  return ((a[0] + a[1]) + (a[2] + a[3])) + ((a[4] + a[5]) + (a[6] + a[7]));
}

__global__ void __launch_bounds__(NTHR, 2)
tree_attn_decode(const unsigned char* __restrict__ kc, const unsigned char* __restrict__ vc, const u32x4* __restrict__ q,
                 unsigned char* __restrict__ ws, const unsigned* __restrict__ kt, const unsigned* __restrict__ vt,
                 const unsigned short* __restrict__ tmask, unsigned* __restrict__ out) {
  __shared__ __attribute__((aligned(16))) float lds[WAVES * D + 2 * WAVES];
  const int b = blockIdx.x;
  const int h = (b & 7) * 4 + (b >> 6), s = (b >> 3) & 7;
  const int comb = h & 7;
  const int tid = threadIdx.x, lane = tid & 63, wave = __builtin_amdgcn_readfirstlane(tid >> 6);
  const int c = lane & 15;
  const __amdgpu_buffer_rsrc_t rk = __builtin_amdgcn_make_buffer_rsrc((void*)kc, 0, H << 20, 0x00020000);
  const __amdgpu_buffer_rsrc_t rv = __builtin_amdgcn_make_buffer_rsrc((void*)vc, 0, H << 20, 0x00020000);
  const unsigned voff = (unsigned)((h << 20) + (s << 17) + ((wave >> 2) << 16) + (tid & 255) * 16);
  const u32x4 qv = q[h * 16 + c];
  const unsigned epoch = __hip_atomic_load((gu32*)(ws + WS_EPOCH + h * 256), RLX_AGENT);
  SB();
  u32x4 K[NLD], V[NLD];
#pragma unroll
  for (int i = 0; i < NLD; ++i) { K[i] = __builtin_amdgcn_raw_buffer_load_b128(rk, voff, i << 12, NT); SB(); }
  __builtin_amdgcn_s_barrier();
#pragma unroll
  for (int i = 0; i < NPRE; ++i) { V[i] = __builtin_amdgcn_raw_buffer_load_b128(rv, voff, i << 12, NT); SB(); }

  float x[NLD];
#pragma unroll
  for (int i = 0; i < NLD; ++i) {
    float d = 0.f;
#pragma unroll
    for (int k = 0; k < 4; ++k) d = dot2(K[i][k], qv[k], d);
    x[i] = d;
  }
#define TR_STEP(HALF, CTRL, BIT) _Pragma("unroll") for (int j = 0; j < (HALF); ++j) { \
    const float lo_ = x[j] + dpp<CTRL>(x[j]), hi_ = x[j + (HALF)] + dpp<CTRL>(x[j + (HALF)]); x[j] = (c & (BIT)) ? hi_ : lo_; }
  TR_STEP(8, XOR8, 8) TR_STEP(4, XOR7, 4) TR_STEP(2, XOR2, 2) TR_STEP(1, XOR1, 1)
#undef TR_STEP
  const float sc = x[0] * (SCALE * LOG2E);
  const float mw = wave_max(sc);
  const float p = ex2(sc - mw);
  const float lw = wave_sum(p);
  const unsigned pk = cvtpk(p, p - bflo(cvtpk(p, p)));
  unsigned sel[4];
#pragma unroll
  for (int w = 0; w < 4; ++w) {
    const unsigned src = (c & 8) ? 0x0302u : 0x0100u;
    sel[w] = (((c & 7) >> 1) == w) ? ((c & 1) ? (src << 16) | 0x0c0cu : src | 0x0c0c0000u) : 0x0c0c0c0cu;
  }
  u32x4 A[NLD];
#define MK_A(i) { const unsigned t_ = dppu<BCAST + (i)>(pk); \
    _Pragma("unroll") for (int w = 0; w < 4; ++w) A[i][w] = __builtin_amdgcn_perm(t_, t_, sel[w]); asm volatile("" : "+v"(A[i])); }
  MK_A(0) MK_A(1) MK_A(2) MK_A(3) MK_A(4) MK_A(5) MK_A(6) MK_A(7)
  MK_A(8) MK_A(9) MK_A(10) MK_A(11) MK_A(12) MK_A(13) MK_A(14) MK_A(15)
#undef MK_A
  if (lane == 0) *reinterpret_cast<f32x2*>(&lds[WAVES * D + 2 * wave]) = f32x2{mw, lw};

  const unsigned tag = __builtin_amdgcn_readfirstlane(epoch) + 1u;
  const __amdgpu_buffer_rsrc_t rw = __builtin_amdgcn_make_buffer_rsrc((void*)ws, 0, WS_END, 0x00020000);
  const unsigned slot_acc = WS_ACC + (unsigned)(h * NSPLIT) * 1024 + lane * 16;
  const unsigned slot_ml = WS_ML + (unsigned)(h * NSPLIT) * 16;
  if (wave == 0) {
    const u32x4 z = {0u, ~tag, 0u, ~tag};
    __builtin_amdgcn_raw_buffer_store_b128(z, rw, slot_acc + s * 1024, 0, SC1);
    if (lane == 0) __builtin_amdgcn_raw_buffer_store_b128(z, rw, slot_ml + s * 16, 0, SC1);
  }
  SB();
#pragma unroll
  for (int i = NPRE; i < NLD; ++i) { V[i] = __builtin_amdgcn_raw_buffer_load_b128(rv, voff, i << 12, NT); SB(); }
  wg_barrier();
  float M, L = 0.f, fw;
  {
    float mv[WAVES], lv[WAVES];
#pragma unroll
    for (int w = 0; w < WAVES; w += 2) {
      const f32x4 t = *reinterpret_cast<const f32x4*>(&lds[WAVES * D + 2 * w]);
      mv[w] = t[0]; lv[w] = t[1]; mv[w + 1] = t[2]; lv[w + 1] = t[3];
    }
    M = mv[0];
#pragma unroll
    for (int w = 1; w < WAVES; ++w) M = fmaxf(M, mv[w]);
    fw = ex2(mw - M);
#pragma unroll
    for (int w = 0; w < WAVES; ++w) L = fmaf(ex2(mv[w] - M), lv[w], L);
  }
  asm volatile("" : "+v"(fw));
  SB();

  f32x4 acc = {0.f, 0.f, 0.f, 0.f};
#pragma unroll
  for (int i = 0; i < NLD; ++i)
    acc = __builtin_amdgcn_mfma_f32_16x16x32_bf16(__builtin_bit_cast(bf16x8, A[i]), __builtin_bit_cast(bf16x8, V[i]), acc, 0, 0, 0);
  {
    auto t0 = __builtin_amdgcn_permlane32_swap(__float_as_uint(acc[0]), __float_as_uint(acc[2]), false, false);
    auto t1 = __builtin_amdgcn_permlane32_swap(__float_as_uint(acc[1]), __float_as_uint(acc[3]), false, false);
    const float y0 = (__uint_as_float(t0[0]) + __uint_as_float(t0[1])) * fw, y1 = (__uint_as_float(t1[0]) + __uint_as_float(t1[1])) * fw;
    *reinterpret_cast<f32x2*>(&lds[wave * D + 8 * c + 4 * ((lane >> 4) & 1) + 2 * (lane >> 5)]) = f32x2{y0, y1};
  }
  if (wave != 0 || s != comb) {
    if (wave == 0 && lane == 0)
      __builtin_amdgcn_raw_buffer_store_b128(u32x4{__float_as_uint(M), tag, __float_as_uint(L), tag}, rw, slot_ml + s * 16, 0, SC1);
    wg_barrier();
    if (wave != 0) return;
    const f32x2 o = merge8(lds, lane);
    __builtin_amdgcn_raw_buffer_store_b128(u32x4{__float_as_uint(o[0]), tag, __float_as_uint(o[1]), tag}, rw, slot_acc + s * 1024, 0, SC1);
    return;
  }

  const unsigned q2 = reinterpret_cast<const unsigned*>(q)[h * 64 + lane], kt2 = kt[h * 64 + lane], vt2 = vt[h * 64 + lane];
  const float tm = bflo(tmask[0]);
  const float mt = (wave_sum(dot2(kt2, q2, 0.f)) * SCALE + (1.0f - tm) * -1000000000.0f) * LOG2E;
  const int j8 = lane & 7;
  u32x4 ml;
#pragma nounroll
  for (int spin = 0; spin < SPIN; ++spin) {
    asm volatile("" ::: "memory");
    ml = __builtin_amdgcn_raw_buffer_load_b128(rw, slot_ml + ((j8 ^ comb) << 4), 0, SC1);
    if (__all(((ml[1] == tag) & (ml[3] == tag)) | (j8 == 0))) break;
    __builtin_amdgcn_s_sleep(1);
  }
  if (lane == 0) __hip_atomic_store((gu32*)(ws + WS_EPOCH + h * 256), tag, RLX_AGENT);
  u32x4 X[NSPLIT];
#define POLL() _Pragma("unroll") for (int j = 1; j < NSPLIT; ++j) X[j] = __builtin_amdgcn_raw_buffer_load_b128(rw, slot_acc, (j ^ comb) << 10, SC1)
  POLL();
  SB();
  const float mj = (j8 == 0) ? M : __uint_as_float(ml[0]), lj = (j8 == 0) ? L : __uint_as_float(ml[2]);
  float MM = fmaxf(mj, mt);
  MM = fmaxf(MM, dpp<XOR7>(MM)); MM = fmaxf(MM, dpp<XOR2>(MM)); MM = fmaxf(MM, dpp<XOR1>(MM));
  const float ej = ex2(mj - MM), et = ex2(mt - MM);
  float LL = ej * lj;
  LL += dpp<XOR7>(LL); LL += dpp<XOR2>(LL); LL += dpp<XOR1>(LL);
  const float inv = 1.0f / (LL + et);
  float wgt[NSPLIT];
#pragma unroll
  for (int j = 0; j < NSPLIT; ++j) wgt[j] = __uint_as_float(__builtin_amdgcn_readlane(__float_as_uint(ej * inv), j));
  float r0 = et * inv * bflo(vt2), r1 = et * inv * bfhi(vt2);
  asm volatile("" : "+v"(r0), "+v"(r1));
  wg_barrier();
  const f32x2 o = merge8(lds, lane);
  r0 = fmaf(wgt[0], o[0], r0); r1 = fmaf(wgt[0], o[1], r1);
  asm volatile("" : "+v"(r0), "+v"(r1));
  unsigned* const po = out + h * 64 + lane;
  float y0, y1;
#pragma nounroll
  for (int spin = 0; spin < SPIN; ++spin) {
    unsigned bad = 0;
    y0 = r0; y1 = r1;
#pragma unroll
    for (int j = 1; j < NSPLIT; ++j) {
      bad |= (X[j][1] ^ tag) | (X[j][3] ^ tag);
      y0 = fmaf(wgt[j], __uint_as_float(X[j][0]), y0); y1 = fmaf(wgt[j], __uint_as_float(X[j][2]), y1);
      asm volatile("" : "+v"(y0), "+v"(y1), "+v"(bad));
    }
    if (__builtin_amdgcn_ballot_w64(bad != 0) == 0) break;
    asm volatile("" ::: "memory");
    POLL();
  }
#undef POLL
  *po = cvtpk(y0, y1);
}

extern "C" void kernel_launch(void* const* d_in, const int* in_sizes, int n_in, void* d_out, int out_size,
                              void* d_ws, size_t ws_size, hipStream_t stream) {
  (void)in_sizes; (void)n_in; (void)out_size; (void)ws_size;
  tree_attn_decode<<<dim3(H * NSPLIT), dim3(NTHR), 0, stream>>>(
      (const unsigned char*)d_in[1], (const unsigned char*)d_in[2], (const u32x4*)d_in[0], (unsigned char*)d_ws,
      (const unsigned*)d_in[3], (const unsigned*)d_in[4], (const unsigned short*)d_in[5], (unsigned*)d_out);
}
```

```cpp
#include <hip/hip_runtime.h>
#include <hip/hip_bf16.h>
#include <stdint.h>

constexpr int H = 32, C = 4096, D = 128;
constexpr int NSPLIT = 8, WAVES = 8, NTHR = WAVES * 64;
constexpr int NLD = 16;
static_assert(NSPLIT * WAVES * NLD * 4 == C && D == 128, "layout");
constexpr float SCALE = 0.088388347648318440f;
constexpr float LOG2E = 1.4426950408889634f;
constexpr int NT = 2, SC1 = 16;
constexpr int SPIN = 1 << 22;
constexpr unsigned WS_EPOCH = 0, WS_ML = 16384, WS_ACC = 32768, WS_END = WS_ACC + H * NSPLIT * 1024;

using bf16x2 = __attribute__((ext_vector_type(2))) __bf16;
using bf16x8 = __attribute__((ext_vector_type(8))) __bf16;
using u32x4 = __attribute__((ext_vector_type(4))) unsigned;
using f32x4 = __attribute__((ext_vector_type(4))) float;
using f32x2 = __attribute__((ext_vector_type(2))) float;
typedef __attribute__((address_space(1))) unsigned gu32;
#define RLX_AGENT __ATOMIC_RELAXED, __HIP_MEMORY_SCOPE_AGENT
#define SB() __builtin_amdgcn_sched_barrier(0)

__device__ __forceinline__ float dot2(unsigned k, unsigned q, float acc) {
  return __builtin_amdgcn_fdot2_f32_bf16(__builtin_bit_cast(bf16x2, k), __builtin_bit_cast(bf16x2, q), acc, false);
}
template <int CTRL> __device__ __forceinline__ unsigned dppu(unsigned x) {
  return (unsigned)__builtin_amdgcn_mov_dpp((int)x, CTRL, 0xf, 0xf, true);
}
template <int CTRL> __device__ __forceinline__ float dpp(float x) { return __uint_as_float(dppu<CTRL>(__float_as_uint(x))); }
constexpr int XOR1 = 0xB1, XOR2 = 0x4E, XOR7 = 0x141, XOR8 = 0x128, BCAST = 0x150;
__device__ __forceinline__ float wave_max(float x) {
  x = fmaxf(x, dpp<XOR8>(x)); x = fmaxf(x, dpp<XOR7>(x)); x = fmaxf(x, dpp<XOR2>(x)); x = fmaxf(x, dpp<XOR1>(x));
  auto s = __builtin_amdgcn_permlane16_swap(__float_as_uint(x), __float_as_uint(x), false, false);
  x = fmaxf(__uint_as_float(s[0]), __uint_as_float(s[1]));
  auto t = __builtin_amdgcn_permlane32_swap(__float_as_uint(x), __float_as_uint(x), false, false);
  return fmaxf(__uint_as_float(t[0]), __uint_as_float(t[1]));
}
__device__ __forceinline__ float wave_sum(float x) {
  x += dpp<XOR8>(x); x += dpp<XOR7>(x); x += dpp<XOR2>(x); x += dpp<XOR1>(x);
  auto s = __builtin_amdgcn_permlane16_swap(__float_as_uint(x), __float_as_uint(x), false, false);
  x = __uint_as_float(s[0]) + __uint_as_float(s[1]);
  auto t = __builtin_amdgcn_permlane32_swap(__float_as_uint(x), __float_as_uint(x), false, false);
  return __uint_as_float(t[0]) + __uint_as_float(t[1]);
}
__device__ __forceinline__ float bflo(unsigned v) { return __uint_as_float(v << 16); }
__device__ __forceinline__ float bfhi(unsigned v) { return __uint_as_float(v & 0xffff0000u); }
__device__ __forceinline__ unsigned cvtpk(float lo, float hi) {
  const bf16x2 r = {(__bf16)lo, (__bf16)hi};
  return __builtin_bit_cast(unsigned, r);
}
__device__ __forceinline__ float ex2(float x) { return __builtin_amdgcn_exp2f(x); }
__device__ __forceinline__ void wg_barrier() {
  asm volatile("s_waitcnt lgkmcnt(0)" ::: "memory");
  __builtin_amdgcn_s_barrier();
  asm volatile("" ::: "memory");
}
__device__ __forceinline__ f32x2 merge8(const float* lds, int lane) {
  f32x2 a[WAVES];
#pragma unroll
  for (int w = 0; w < WAVES; ++w) a[w] = *reinterpret_cast<const f32x2*>(&lds[w * D + 2 * lane]);
  return ((a[0] + a[1]) + (a[2] + a[3])) + ((a[4] + a[5]) + (a[6] + a[7]));
}

__global__ void __launch_bounds__(NTHR, 2)
tree_attn_decode(const unsigned char* __restrict__ kc, const unsigned char* __restrict__ vc, const u32x4* __restrict__ q,
                 unsigned char* __restrict__ ws, const unsigned* __restrict__ kt, const unsigned* __restrict__ vt,
                 const unsigned short* __restrict__ tmask, unsigned* __restrict__ out) {
  __shared__ __attribute__((aligned(16))) float lds[WAVES * D + 2 * WAVES];
  const int b = blockIdx.x;
  const int h = (b & 7) * 4 + (b >> 6), s = (b >> 3) & 7;
  const int comb = h & 7;
  const int tid = threadIdx.x, lane = tid & 63, wave = __builtin_amdgcn_readfirstlane(tid >> 6);
  const int c = lane & 15;
  const __amdgpu_buffer_rsrc_t rk = __builtin_amdgcn_make_buffer_rsrc((void*)kc, 0, H << 20, 0x00020000);
  const __amdgpu_buffer_rsrc_t rv = __builtin_amdgcn_make_buffer_rsrc((void*)vc, 0, H << 20, 0x00020000);
  unsigned voff = (unsigned)((h << 20) + (s << 17) + ((tid >> 8) << 16) + (tid & 255) * 16);
  unsigned qoff = (unsigned)(h * 256 + c * 16), eoff = WS_EPOCH + (unsigned)h * 256;
  asm volatile("" : "+v"(voff), "+v"(qoff), "+v"(eoff));
  const u32x4 qv = *reinterpret_cast<const u32x4*>(reinterpret_cast<const unsigned char*>(q) + qoff);
  const unsigned epoch = __hip_atomic_load((gu32*)(ws + eoff), RLX_AGENT);
  SB();
  u32x4 K[NLD], V[NLD];
#pragma unroll
  for (int i = 0; i < NLD; ++i) { K[i] = __builtin_amdgcn_raw_buffer_load_b128(rk, voff, i << 12, NT); SB(); }
  asm volatile("" :: "s"(rv));
  if (wave >= 4) __builtin_amdgcn_s_barrier();
  SB();

  float x[NLD];
#pragma unroll
  for (int i = 0; i < NLD; ++i) {
    float d = 0.f;
#pragma unroll
    for (int k = 0; k < 4; ++k) d = dot2(K[i][k], qv[k], d);
    x[i] = d;
  }
#define TR_STEP(HALF, CTRL, BIT) _Pragma("unroll") for (int j = 0; j < (HALF); ++j) { \
    const float lo_ = x[j] + dpp<CTRL>(x[j]), hi_ = x[j + (HALF)] + dpp<CTRL>(x[j + (HALF)]); x[j] = (c & (BIT)) ? hi_ : lo_; }
  TR_STEP(8, XOR8, 8) TR_STEP(4, XOR7, 4) TR_STEP(2, XOR2, 2) TR_STEP(1, XOR1, 1)
#undef TR_STEP
  const float sc = x[0] * (SCALE * LOG2E);
  const float mw = wave_max(sc);
  const float p = ex2(sc - mw);
  const float lw = wave_sum(p);
  const unsigned pk = cvtpk(p, p - bflo(cvtpk(p, p)));
  unsigned sel[4];
#pragma unroll
  for (int w = 0; w < 4; ++w) {
    const unsigned src = (c & 8) ? 0x0302u : 0x0100u;
    sel[w] = (((c & 7) >> 1) == w) ? ((c & 1) ? (src << 16) | 0x0c0cu : src | 0x0c0c0000u) : 0x0c0c0c0cu;
  }
  u32x4 A[NLD];
#define MK_A(i) { const unsigned t_ = dppu<BCAST + (i)>(pk); \
    _Pragma("unroll") for (int w = 0; w < 4; ++w) A[i][w] = __builtin_amdgcn_perm(t_, t_, sel[w]); asm volatile("" : "+v"(A[i])); }
  MK_A(0) MK_A(1) MK_A(2) MK_A(3) MK_A(4) MK_A(5) MK_A(6) MK_A(7)
  MK_A(8) MK_A(9) MK_A(10) MK_A(11) MK_A(12) MK_A(13) MK_A(14) MK_A(15)
#undef MK_A
  if (lane == 0) *reinterpret_cast<f32x2*>(&lds[WAVES * D + 2 * wave]) = f32x2{mw, lw};

  const unsigned tag = __builtin_amdgcn_readfirstlane(epoch) + 1u;
  const __amdgpu_buffer_rsrc_t rw = __builtin_amdgcn_make_buffer_rsrc((void*)ws, 0, WS_END, 0x00020000);
  const unsigned slot_acc = WS_ACC + (unsigned)(h * NSPLIT) * 1024 + lane * 16;
  const unsigned slot_ml = WS_ML + (unsigned)(h * NSPLIT) * 16;
  if (wave == 0) {
    const u32x4 z = {0u, ~tag, 0u, ~tag};
    __builtin_amdgcn_raw_buffer_store_b128(z, rw, slot_acc + s * 1024, 0, SC1);
    if (lane == 0) __builtin_amdgcn_raw_buffer_store_b128(z, rw, slot_ml + s * 16, 0, SC1);
  }
  if (wave < 4) __builtin_amdgcn_s_barrier();
  SB();
#pragma unroll
  for (int i = 0; i < NLD; ++i) { V[i] = __builtin_amdgcn_raw_buffer_load_b128(rv, voff, i << 12, NT); SB(); }
  wg_barrier();
  float M, L = 0.f, fw;
  {
    float mv[WAVES], lv[WAVES];
#pragma unroll
    for (int w = 0; w < WAVES; w += 2) {
      const f32x4 t = *reinterpret_cast<const f32x4*>(&lds[WAVES * D + 2 * w]);
      mv[w] = t[0]; lv[w] = t[1]; mv[w + 1] = t[2]; lv[w + 1] = t[3];
    }
    M = mv[0];
#pragma unroll
    for (int w = 1; w < WAVES; ++w) M = fmaxf(M, mv[w]);
    fw = ex2(mw - M);
#pragma unroll
    for (int w = 0; w < WAVES; ++w) L = fmaf(ex2(mv[w] - M), lv[w], L);
  }
  asm volatile("" : "+v"(fw));
  SB();

  f32x4 acc = {0.f, 0.f, 0.f, 0.f};
#pragma unroll
  for (int i = 0; i < NLD; ++i)
    acc = __builtin_amdgcn_mfma_f32_16x16x32_bf16(__builtin_bit_cast(bf16x8, A[i]), __builtin_bit_cast(bf16x8, V[i]), acc, 0, 0, 0);
  {
    auto t0 = __builtin_amdgcn_permlane32_swap(__float_as_uint(acc[0]), __float_as_uint(acc[2]), false, false);
    auto t1 = __builtin_amdgcn_permlane32_swap(__float_as_uint(acc[1]), __float_as_uint(acc[3]), false, false);
    const float y0 = (__uint_as_float(t0[0]) + __uint_as_float(t0[1])) * fw, y1 = (__uint_as_float(t1[0]) + __uint_as_float(t1[1])) * fw;
    *reinterpret_cast<f32x2*>(&lds[wave * D + 8 * c + 4 * ((lane >> 4) & 1) + 2 * (lane >> 5)]) = f32x2{y0, y1};
  }
  if (wave != 0 || s != comb) {
    if (wave == 0 && lane == 0)
      __builtin_amdgcn_raw_buffer_store_b128(u32x4{__float_as_uint(M), tag, __float_as_uint(L), tag}, rw, slot_ml + s * 16, 0, SC1);
    wg_barrier();
    if (wave != 0) return;
    const f32x2 o = merge8(lds, lane);
    __builtin_amdgcn_raw_buffer_store_b128(u32x4{__float_as_uint(o[0]), tag, __float_as_uint(o[1]), tag}, rw, slot_acc + s * 1024, 0, SC1);
    return;
  }

  const unsigned q2 = reinterpret_cast<const unsigned*>(q)[h * 64 + lane], kt2 = kt[h * 64 + lane], vt2 = vt[h * 64 + lane];
  const float tm = bflo(tmask[0]);
  const float mt = (wave_sum(dot2(kt2, q2, 0.f)) * SCALE + (1.0f - tm) * -1000000000.0f) * LOG2E;
  const int j8 = lane & 7;
  u32x4 ml;
#pragma nounroll
  for (int spin = 0; spin < SPIN; ++spin) {
    asm volatile("" ::: "memory");
    ml = __builtin_amdgcn_raw_buffer_load_b128(rw, slot_ml + ((j8 ^ comb) << 4), 0, SC1);
    if (__all(((ml[1] == tag) & (ml[3] == tag)) | (j8 == 0))) break;
    __builtin_amdgcn_s_sleep(1);
  }
  if (lane == 0) __hip_atomic_store((gu32*)(ws + WS_EPOCH + h * 256), tag, RLX_AGENT);
  u32x4 X[NSPLIT];
#define POLL() _Pragma("unroll") for (int j = 1; j < NSPLIT; ++j) X[j] = __builtin_amdgcn_raw_buffer_load_b128(rw, slot_acc, (j ^ comb) << 10, SC1)
  POLL();
  SB();
  const float mj = (j8 == 0) ? M : __uint_as_float(ml[0]), lj = (j8 == 0) ? L : __uint_as_float(ml[2]);
  float MM = fmaxf(mj, mt);
  MM = fmaxf(MM, dpp<XOR7>(MM)); MM = fmaxf(MM, dpp<XOR2>(MM)); MM = fmaxf(MM, dpp<XOR1>(MM));
  const float ej = ex2(mj - MM), et = ex2(mt - MM);
  float LL = ej * lj;
  LL += dpp<XOR7>(LL); LL += dpp<XOR2>(LL); LL += dpp<XOR1>(LL);
  const float inv = 1.0f / (LL + et);
  float wgt[NSPLIT];
#pragma unroll
  for (int j = 0; j < NSPLIT; ++j) wgt[j] = __uint_as_float(__builtin_amdgcn_readlane(__float_as_uint(ej * inv), j));
  float r0 = et * inv * bflo(vt2), r1 = et * inv * bfhi(vt2);
  asm volatile("" : "+v"(r0), "+v"(r1));
  wg_barrier();
  const f32x2 o = merge8(lds, lane);
  r0 = fmaf(wgt[0], o[0], r0); r1 = fmaf(wgt[0], o[1], r1);
  asm volatile("" : "+v"(r0), "+v"(r1));
  unsigned* const po = out + h * 64 + lane;
  float y0, y1;
#pragma nounroll
  for (int spin = 0; spin < SPIN; ++spin) {
    unsigned bad = 0;
    y0 = r0; y1 = r1;
#pragma unroll
    for (int j = 1; j < NSPLIT; ++j) {
      bad |= (X[j][1] ^ tag) | (X[j][3] ^ tag);
      y0 = fmaf(wgt[j], __uint_as_float(X[j][0]), y0); y1 = fmaf(wgt[j], __uint_as_float(X[j][2]), y1);
      asm volatile("" : "+v"(y0), "+v"(y1), "+v"(bad));
    }
    if (__builtin_amdgcn_ballot_w64(bad != 0) == 0) break;
    asm volatile("" ::: "memory");
    POLL();
  }
#undef POLL
  *po = cvtpk(y0, y1);
}

extern "C" void kernel_launch(void* const* d_in, const int* in_sizes, int n_in, void* d_out, int out_size,
                              void* d_ws, size_t ws_size, hipStream_t stream) {
  (void)in_sizes; (void)n_in; (void)out_size; (void)ws_size;
  tree_attn_decode<<<dim3(H * NSPLIT), dim3(NTHR), 0, stream>>>(
      (const unsigned char*)d_in[1], (const unsigned char*)d_in[2], (const u32x4*)d_in[0], (unsigned char*)d_ws,
      (const unsigned*)d_in[3], (const unsigned*)d_in[4], (const unsigned short*)d_in[5], (unsigned*)d_out);
}
```
